```python
import torch

def _softmax(x, axis=-1):
    return torch.softmax(x, dim=axis)
def _gelu(x):
    return torch.nn.functional.gelu(x, approximate="tanh")
def _silu(x):
    return torch.nn.functional.silu(x)
def _sigmoid(x):
    return torch.sigmoid(x)
def _layernorm(x, axis=-1, eps=1e-5):
    m = x.mean(dim=axis, keepdim=True)
    v = x.var(dim=axis, keepdim=True, unbiased=False)
    return (x - m) / torch.sqrt(v + eps)
def _scatter_add(tgt, idx, src, axis=0):
    return tgt.clone().index_add_(axis, idx, src)
def _segment_sum(src, seg, n):
    out = torch.zeros((n,) + src.shape[1:], dtype=src.dtype)
    return out.index_add_(0, seg, src)
def _pad(x, pw):
    flat = tuple(v for pair in reversed(pw) for v in pair)
    return torch.nn.functional.pad(x, flat)

kernel_name = 'gather_scatter__hybrid_graph_tf_nagphormer__graph'

def setup_inputs():

    _w = (lambda a: (lambda t: t.to(torch.bfloat16) if t.is_floating_point() else t)(torch.as_tensor(a)))
    N, deg, D = 25000, 32, 64
    E = N * deg
    src = (lambda lo, hi, *s: torch.randint(lo, hi, s))(0, N, E).to(torch.int64)
    dst = (lambda lo, hi, *s: torch.randint(lo, hi, s))(0, N, E).to(torch.int64)
    deg_cnt = torch.bincount(dst, minlength=N).to(float) + 1.0
    sc = 1.0 / D ** 0.5
    return {
        "x": _w((lambda *s: torch.randn(*s, dtype=torch.bfloat16) * 0.1)(N, D)),
        "src": _w(src),
        "dst": _w(dst),
        "deg_inv_sqrt": _w(1.0 / (lambda _x: torch.sqrt(torch.as_tensor(_x)))(deg_cnt)),
        "wq": _w((lambda *s: torch.randn(*s, dtype=torch.bfloat16) * 1.6)(D, D) * sc),
        "wk": _w((lambda *s: torch.randn(*s, dtype=torch.bfloat16) * 3.2)(D, D) * sc),
        "wv": _w((lambda *s: torch.randn(*s, dtype=torch.bfloat16) * 0.1)(D, D) * sc),
        "wo": _w((lambda *s: torch.randn(*s, dtype=torch.bfloat16) * 0.1)(D, D) * sc),
        "w_r": _w((lambda *s: torch.randn(*s, dtype=torch.bfloat16) * 0.1)(D)),
    }

def reference(x, src, dst, deg_inv_sqrt, wq, wk, wv, wo, w_r):
    N, D = x.shape[0], x.shape[1]
    K = 7
    h = x
    toks = [x]
    for _ in range(K):
        msg = h[src] * deg_inv_sqrt[src, None] * deg_inv_sqrt[dst, None]
        h = _segment_sum(msg, dst, N)
        toks.append(h)
    tok = torch.stack(toks, axis=1)
    t = _layernorm(tok, axis=-1)
    q   = torch.einsum('nkd,de->nke', t, wq)
    key = torch.einsum('nkd,de->nke', t, wk)
    v   = torch.einsum('nkd,de->nke', t, wv)
    scale = 1.0 / (D ** 0.5)
    attn = _softmax(torch.einsum('nkd,nld->nkl', q, key) * scale, axis=-1)
    o = torch.einsum('nkl,nld->nkd', attn, v)
    o = tok + torch.einsum('nkd,de->nke', o, wo)
    r = _layernorm(o, axis=-1)
    alpha = _softmax(torch.einsum('nkd,d->nk', r, w_r), axis=-1)
    return torch.einsum('nk,nkd->nd', alpha, o)
```

```cpp
#include <hip/hip_runtime.h>
#include <hip/hip_bf16.h>
#include <stdint.h>

typedef unsigned short u16;
typedef unsigned int u32;

__device__ __forceinline__ float bf2f(u16 v) { return __uint_as_float(((u32)v) << 16); }
__device__ __forceinline__ u16 f2bf(float f) {
    __hip_bfloat16 b = __float2bfloat16(f);
    return *reinterpret_cast<u16*>(&b);
}

__global__ void k_hist(const int* dst, int E, int* cnt) {
    int i = blockIdx.x * blockDim.x + threadIdx.x;
    if (i < E) atomicAdd(&cnt[dst[i]], 1);
}
__global__ void k_scan(const int* cnt, int N, int* rowptr, int* cursor) {
    __shared__ int part[1024];
    int t = threadIdx.x;
    int per = (N + 1023) / 1024;
    int s = 0;
    for (int i = t * per; i < min(N, (t + 1) * per); ++i) s += cnt[i];
    part[t] = s;
    __syncthreads();
    for (int o = 1; o < 1024; o <<= 1) {
        int v = (t >= o) ? part[t - o] : 0;
        __syncthreads();
        part[t] += v;
        __syncthreads();
    }
    int base = part[t] - s;
    for (int i = t * per; i < min(N, (t + 1) * per); ++i) {
        rowptr[i] = base; cursor[i] = base; base += cnt[i];
    }
    if (t == 1023) rowptr[N] = part[1023];
}
__global__ void k_fill(const int* src, const int* dst, int E, int* cursor, int* col) {
    int i = blockIdx.x * blockDim.x + threadIdx.x;
    if (i < E) { int p = atomicAdd(&cursor[dst[i]], 1); col[p] = src[i]; }
}
__global__ void k_g0(const u16* x, const u16* dis, int N, u16* g) {
    int i = blockIdx.x * blockDim.x + threadIdx.x;
    if (i < N * 64) g[i] = f2bf(bf2f(x[i]) * bf2f(dis[i >> 6]));
}
__global__ void k_hop(const u16* gin, const int* rowptr, const int* col, const u16* dis, int N, u16* hout, u16* gout) {
    int w = (blockIdx.x * blockDim.x + threadIdx.x) >> 6;
    int lane = threadIdx.x & 63;
    if (w >= N) return;
    float acc = 0.f;
    for (int e = rowptr[w]; e < rowptr[w + 1]; ++e) acc += bf2f(gin[(size_t)col[e] * 64 + lane]);
    float d = bf2f(dis[w]);
    u16 h = f2bf(acc * d);
    hout[(size_t)w * 64 + lane] = h;
    gout[(size_t)w * 64 + lane] = f2bf(bf2f(h) * d);
}
__device__ __forceinline__ float wsum(float v) {
    for (int o = 32; o; o >>= 1) v += __shfl_xor(v, o);
    return v;
}
__global__ void __launch_bounds__(256) k_final(const u16* x, const u16* hs, int N, const u16* wq, const u16* wk, const u16* wv, const u16* wo, const u16* wr, u16* out) {
    __shared__ float sw[4][64 * 64];
    __shared__ float st[4][8][64];
    __shared__ float sq[4][8][64], sk[4][8][64];
    for (int i = threadIdx.x; i < 4096; i += 256) {
        sw[0][i] = bf2f(wq[i]); sw[1][i] = bf2f(wk[i]); sw[2][i] = bf2f(wv[i]); sw[3][i] = bf2f(wo[i]);
    }
    __syncthreads();
    int wid = threadIdx.x >> 6, lane = threadIdx.x & 63;
    float wrl = bf2f(wr[lane]);
    for (int n0 = blockIdx.x * 4; n0 < N; n0 += gridDim.x * 4) {
        int n = n0 + wid;
        bool ok = n < N;
        int nn = ok ? n : N - 1;
        float tok[8];
        for (int k = 0; k < 8; ++k) {
            const u16* p = (k == 0) ? x : hs + (size_t)(k - 1) * N * 64;
            tok[k] = bf2f(p[(size_t)nn * 64 + lane]);
            float m = wsum(tok[k]) * (1.f / 64);
            float c = tok[k] - m;
            float v = wsum(c * c) * (1.f / 64);
            st[wid][k][lane] = c / sqrtf(v + 1e-5f);
        }
        __syncthreads();
        float q[8], kk[8], vv[8];
        for (int k = 0; k < 8; ++k) { q[k] = 0; kk[k] = 0; vv[k] = 0; }
        for (int d = 0; d < 64; ++d) {
            float a = sw[0][d * 64 + lane], b = sw[1][d * 64 + lane], c = sw[2][d * 64 + lane];
            for (int k = 0; k < 8; ++k) { float t = st[wid][k][d]; q[k] += t * a; kk[k] += t * b; vv[k] += t * c; }
        }
        float o[8];
        for (int k = 0; k < 8; ++k) {
            float s[8]; float mx = -1e30f;
            for (int l = 0; l < 8; ++l) { s[l] = wsum(q[k] * kk[l]) * 0.125f; mx = fmaxf(mx, s[l]); }
            float sum = 0;
            for (int l = 0; l < 8; ++l) { s[l] = __expf(s[l] - mx); sum += s[l]; }
            float acc = 0;
            for (int l = 0; l < 8; ++l) acc += s[l] * vv[l];
            o[k] = acc / sum;
        }
        __syncthreads();
        for (int k = 0; k < 8; ++k) st[wid][k][lane] = o[k];
        __syncthreads();
        float oo[8];
        for (int k = 0; k < 8; ++k) oo[k] = 0;
        for (int d = 0; d < 64; ++d) {
            float a = sw[3][d * 64 + lane];
            for (int k = 0; k < 8; ++k) oo[k] += st[wid][k][d] * a;
        }
        float z[8]; float mx = -1e30f;
        for (int k = 0; k < 8; ++k) {
            oo[k] += tok[k];
            float m = wsum(oo[k]) * (1.f / 64);
            float c = oo[k] - m;
            float v = wsum(c * c) * (1.f / 64);
            float r = c / sqrtf(v + 1e-5f);
            z[k] = wsum(r * wrl);
            mx = fmaxf(mx, z[k]);
        }
        float sum = 0;
        for (int k = 0; k < 8; ++k) { z[k] = __expf(z[k] - mx); sum += z[k]; }
        float res = 0;
        for (int k = 0; k < 8; ++k) res += z[k] * oo[k];
        res /= sum;
        if (ok) out[(size_t)n * 64 + lane] = f2bf(res);
        __syncthreads();
    }
}

extern "C" void kernel_launch(void* const* d_in, const int* in_sizes, int n_in,
                              void* d_out, int out_size, void* d_ws, size_t ws_size,
                              hipStream_t stream) {
    const u16* x = (const u16*)d_in[0];
    const int* src = (const int*)d_in[1];
    const int* dst = (const int*)d_in[2];
    const u16* dis = (const u16*)d_in[3];
    const u16* wq = (const u16*)d_in[4];
    const u16* wk = (const u16*)d_in[5];
    const u16* wv = (const u16*)d_in[6];
    const u16* wo = (const u16*)d_in[7];
    const u16* wr = (const u16*)d_in[8];
    int N = in_sizes[3];
    int E = in_sizes[1];
    char* ws = (char*)d_ws;
    size_t off = 0;
    auto alloc = [&](size_t bytes) { void* p = ws + off; off += (bytes + 255) & ~(size_t)255; return p; };
    int* cnt = (int*)alloc((size_t)N * 4);
    int* rowptr = (int*)alloc((size_t)(N + 1) * 4);
    int* cursor = (int*)alloc((size_t)N * 4);
    int* col = (int*)alloc((size_t)E * 4);
    u16* ga = (u16*)alloc((size_t)N * 128);
    u16* gb = (u16*)alloc((size_t)N * 128);
    u16* hs = (u16*)alloc((size_t)N * 128 * 7);
    hipMemsetAsync(cnt, 0, (size_t)N * 4, stream);
    k_hist<<<(E + 255) / 256, 256, 0, stream>>>(dst, E, cnt);
    k_scan<<<1, 1024, 0, stream>>>(cnt, N, rowptr, cursor);
    k_fill<<<(E + 255) / 256, 256, 0, stream>>>(src, dst, E, cursor, col);
    k_g0<<<(N * 64 + 255) / 256, 256, 0, stream>>>(x, dis, N, ga);
    for (int k = 0; k < 7; ++k) {
        k_hop<<<(N + 3) / 4, 256, 0, stream>>>(ga, rowptr, col, dis, N, hs + (size_t)k * N * 64, gb);
        u16* t = ga; ga = gb; gb = t;
    }
    k_final<<<512, 256, 0, stream>>>(x, hs, N, wq, wk, wv, wo, wr, (u16*)d_out);
}
```

```cpp
#define KP 4
#define CP 3
#define TH 20000
#include <hip/hip_runtime.h>
#include <stdint.h>
#include <stddef.h>

typedef unsigned short u16;
typedef unsigned int u32;
typedef unsigned long long u64;
typedef u32 u32x4 __attribute__((ext_vector_type(4)));
typedef u32 u32x2 __attribute__((ext_vector_type(2)));
typedef u32 u32x16 __attribute__((ext_vector_type(16)));
typedef float f32x4 __attribute__((ext_vector_type(4)));
typedef __bf16 bf8 __attribute__((ext_vector_type(8)));
typedef __bf16 bf2 __attribute__((ext_vector_type(2)));
typedef short s16x4 __attribute__((ext_vector_type(4)));

constexpr int NSLOT = 4;

constexpr int NWG = 256;
constexpr int NPB = 98;
#define GW 15
constexpr int NGW = GW;
constexpr int MAXB = 12;
constexpr int CAPU = MAXB * 16;
constexpr int OVCAP = 384;
constexpr int HOPS = 7;
constexpr int CELL = 24;
constexpr int OVH = 15;
constexpr unsigned OVL = 0xf00000u;
#define SRDT HOPS
#define FA 16
#define BA 0
#define KB 1
#define FD 0
#define K0 0
#define K0P 1
#define KP1 KP
#define BF 0
#define FPOS 0
#define CA 0
#define OA 0
#define LTB TS
#define BUDGET 400000u
#define FORCEGEN
constexpr int SCAP = 64;
constexpr u32 PADOFF = 0x7fffff00u;
constexpr u32 PADE = 0x80000000u;
constexpr u32 SENT = 0xffffffffu;
constexpr u32 TS = 1u << 22;

__device__ __forceinline__ float bf2f(u16 v) { return __uint_as_float(((u32)v) << 16); }
__device__ __forceinline__ float blo(u32 v) { return __uint_as_float(v << 16); }
__device__ __forceinline__ float bhi(u32 v) { return __uint_as_float(v & 0xffff0000u); }
__device__ __forceinline__ u32 pk(float a, float b) { bf2 r; r.x = (__bf16)a; r.y = (__bf16)b; return __builtin_bit_cast(u32, r); }
__device__ __forceinline__ u16 f2bf(float a) { return (u16)(pk(a, 0.f) & 0xffffu); }
__device__ __forceinline__ __amdgpu_buffer_rsrc_t mkrs(const void* p, u32 bytes) {
    return __builtin_amdgcn_make_buffer_rsrc((void*)p, 0, bytes, 0x00020000);
}
__device__ __forceinline__ u32 scale2(u32 w, float d) { return min(pk(blo(w) * d, bhi(w) * d), 0xfffffffeu); }
__device__ __forceinline__ u32 ldsaddr(const void* p) { return (u32)(uintptr_t)(const __attribute__((address_space(3))) void*)p; }
__device__ __forceinline__ u32 max4(u32x4 v) { return max(max(v[0], v[1]), max(v[2], v[3])); }
constexpr u32 DIVM = 171197u;
__device__ __forceinline__ int partbase(int p) { return (p * 49 + 1) >> 1; }
template <int CTRL, int RM> __device__ __forceinline__ u32 dppu(u32 v) { return __builtin_amdgcn_update_dpp(0u, v, CTRL, RM, 0xf, false); }
__device__ __forceinline__ u32 scan16(u32 v) { v += dppu<0x111, 15>(v); v += dppu<0x112, 15>(v); v += dppu<0x114, 15>(v); v += dppu<0x118, 15>(v); return v; }
__device__ __forceinline__ u32 scan64(u32 v) { v = scan16(v); v += dppu<0x142, 10>(v); v += dppu<0x143, 12>(v); return v; }
__device__ __forceinline__ float xsum1632(float v) {
    u32 a = __float_as_uint(v);
    auto r = __builtin_amdgcn_permlane16_swap(a, a, false, false);
    v = __uint_as_float(r[0]) + __uint_as_float(r[1]);
    a = __float_as_uint(v);
    auto q = __builtin_amdgcn_permlane32_swap(a, a, false, false);
    return __uint_as_float(q[0]) + __uint_as_float(q[1]);
}
template <int CTRL> __device__ __forceinline__ float dpp(float v) {
    return __uint_as_float(__builtin_amdgcn_update_dpp(0u, __float_as_uint(v), CTRL, 0xf, 0xf, true));
}
__device__ __forceinline__ float sum8(float v) { v += dpp<0xb1>(v); v += dpp<0x4e>(v); v += dpp<0x141>(v); return v; }
__device__ __forceinline__ float max8(float v) {
    float m;
    asm("s_nop 1\n\tv_max_f32_dpp %0, %1, %1 quad_perm:[1,0,3,2] row_mask:0xf bank_mask:0xf\n\t"
        "s_nop 1\n\tv_max_f32_dpp %0, %0, %0 quad_perm:[2,3,0,1] row_mask:0xf bank_mask:0xf\n\t"
        "s_nop 1\n\tv_max_f32_dpp %0, %0, %0 row_half_mirror row_mask:0xf bank_mask:0xf" : "=&v"(m) : "v"(v));
    return m;
}
__device__ __forceinline__ float max2(float a, float b) { float m; asm("v_max_f32 %0, %1, %2" : "=v"(m) : "v"(a), "v"(b)); return m; }
__device__ __forceinline__ float keep(float a) { asm("" : "+v"(a)); return a; }

template <bool isM>
__device__ __forceinline__ void fuse_weights(const u16* __restrict__ Wa, const u16* __restrict__ Wb, u32x4* frag, int lane) {
    u32x4* const tr = frag;
    const int m = lane & 15, g = lane >> 4;
    const float sc = isM ? 0.125f * 1.44269504088896f : 1.f;
    bf8 A[4][2], B[4][2];
#pragma unroll
    for (int i = 0; i < 4; ++i) {
        const int e = 32 * (i >> 1) + 8 * (m >> 2) + 4 * (i & 1) + (m & 3);
#pragma unroll
        for (int cs = 0; cs < 2; ++cs) {
            A[i][cs] = *(const bf8*)(Wa + e * 64 + 32 * cs + 8 * g);
            if (isM) B[i][cs] = *(const bf8*)(Wb + e * 64 + 32 * cs + 8 * g);
        }
    }
    if (!isM) {
#pragma unroll
        for (int i = 0; i < 8; ++i) tr[i * 64 + lane] = ((const u32x4*)Wb)[i * 64 + lane];
        asm volatile("" ::: "memory");
#pragma unroll
        for (int i = 0; i < 4; ++i) {
            const int e = 32 * (i >> 1) + 8 * (m >> 2) + 4 * (i & 1) + (m & 3);
#pragma unroll
            for (int cs = 0; cs < 2; ++cs)
#pragma unroll
                for (int j = 0; j < 8; ++j) B[i][cs][j] = __builtin_bit_cast(__bf16, ((const u16*)tr)[(32 * cs + 8 * g + j) * 64 + e]);
        }
    }
    f32x4 D[4][4];
#pragma unroll
    for (int et = 0; et < 4; ++et) {
        float cm = 0.f;
#pragma unroll
        for (int sh = 0; sh < 4; ++sh) {
            D[et][sh] = (f32x4){0, 0, 0, 0};
#pragma unroll
            for (int cs = 0; cs < 2; ++cs) D[et][sh] = __builtin_amdgcn_mfma_f32_16x16x32_bf16(A[sh][cs], B[et][cs], D[et][sh], 0, 0, 0);
            cm += (D[et][sh][0] + D[et][sh][1]) + (D[et][sh][2] + D[et][sh][3]);
        }
        cm = xsum1632(cm) * (1.f / 64);
#pragma unroll
        for (int sh = 0; sh < 4; ++sh) D[et][sh] -= cm;
    }
#pragma unroll
    for (int sh = 0; sh < 4; ++sh) {
#pragma unroll
        for (int i = 0; i < 4; ++i) {
            float rm = 0.f;
            if (isM) {
                rm = sum8((D[0][sh][i] + D[1][sh][i]) + (D[2][sh][i] + D[3][sh][i]));
                rm += dpp<0x140>(rm);
                rm *= 1.f / 64;
            }
#pragma unroll
            for (int et = 0; et < 4; ++et) D[et][sh][i] = (D[et][sh][i] - rm) * sc;
        }
    }
#pragma unroll
    for (int et = 0; et < 4; ++et)
#pragma unroll
        for (int s = 0; s < 2; ++s)
            frag[(et * 2 + s) * 64 + lane] = (u32x4){pk(D[et][2 * s][0], D[et][2 * s][1]), pk(D[et][2 * s][2], D[et][2 * s][3]),
                                                     pk(D[et][2 * s + 1][0], D[et][2 * s + 1][1]), pk(D[et][2 * s + 1][2], D[et][2 * s + 1][3])};
}

#define CPN 3
__device__ __forceinline__ void code_prefetch(int lane, bool on, u32 (&v)[CPN]) {
    u64 pc;
    int rel;
    asm volatile("s_getpc_b64 %0\n\ts_mov_b32 %1, KM_begin-." : "=s"(pc), "=s"(rel));
    const __amdgpu_buffer_rsrc_t rs = mkrs((const void*)((pc + rel) & ~127ull), CPN * 8192u);
#pragma unroll
    for (int i = 0; i < CPN; ++i) v[i] = __builtin_amdgcn_raw_buffer_load_b32(rs, on ? lane * 128u : PADOFF, i * 8192, 0);
}
__device__ __forceinline__ void code_done(u32 (&v)[CPN]) {
#pragma unroll
    for (int i = 0; i < CPN; ++i) asm volatile("" :: "v"(v[i]));
}

__global__ void __launch_bounds__(1024, 4)
k_bin(const int* __restrict__ src, const int* __restrict__ dst,
      const u16* __restrict__ x, const u16* __restrict__ dis,
      u32x4* __restrict__ cells, u32* __restrict__ ovf, u32* __restrict__ ovh, u32x4* __restrict__ gtab,
      int E, int chunk, int N
      ) {
#define STB(i)
    STB(0);
    __shared__ u32x4 cell4[NWG * CELL / 4 + 4];
    __shared__ u32 cnt[NWG + 1];
    __shared__ u32 ocnt;
    const int c = blockIdx.x, t = threadIdx.x;
    const int e0 = c * chunk + t * 4;
    const int eend = min(E, (c + 1) * chunk);
    const u32 eo = e0 < eend ? (u32)e0 * 4u : PADOFF;
    const int node = c * NPB + (t >> 3);
    const bool own = t < NPB * 8 && node < N;
    const u32 ro = own ? (u32)node * 128u + (t & 7) * 16u : PADOFF;
    const u32x4 sent = {SENT, SENT, SENT, SENT};
    u32x4 s4, d4, xv;
    u32 dv;
    auto loads = [&]() {
        if (e0 + 4 <= E) {
            s4 = __builtin_amdgcn_raw_buffer_load_b128(mkrs(src, (u32)E * 4u), eo, 0, 0);
            d4 = __builtin_amdgcn_raw_buffer_load_b128(mkrs(dst, (u32)E * 4u), eo, 0, 0);
        } else {
#pragma unroll
            for (int j = 0; j < 4; ++j) {
                s4[j] = __builtin_amdgcn_raw_buffer_load_b32(mkrs(src, (u32)E * 4u), eo + 4 * j, 0, 0);
                d4[j] = __builtin_amdgcn_raw_buffer_load_b32(mkrs(dst, (u32)E * 4u), eo + 4 * j, 0, 0);
            }
        }
        xv = __builtin_amdgcn_raw_buffer_load_b128(mkrs(x, (u32)N * 128u), ro, 0, 0);
        dv = __builtin_amdgcn_raw_buffer_load_b16(mkrs(dis, (u32)N * 2u), own ? (u32)node * 2u : PADOFF, 0, 0);
        __builtin_amdgcn_raw_buffer_store_b128(sent, mkrs((char*)gtab + TS, (u32)N * 128u), ro, 0, BA);
#pragma unroll
        for (int q = 2; q <= KB; ++q) __builtin_amdgcn_raw_buffer_store_b128(sent, mkrs((char*)gtab + (size_t)q * TS, (u32)N * 128u), ro, 0, BF);
    };
    auto init = [&]() {
        cell4[t] = sent;
        if (t < NWG * CELL / 4 + 4 - 1024) cell4[t + 1024] = sent;
        if (t <= NWG) cnt[t] = 0;
        if (t == 0) ocnt = 0;
        asm volatile("s_waitcnt lgkmcnt(0)\n\ts_barrier" ::: "memory");
    };
    loads();
    u32 cpv[CPN];
    code_prefetch(t & 63, t >= 960, cpv);
    init();
    const float dn = bf2f((u16)dv);
    STB(1);
    u32* cell = (u32*)cell4;
    const int pos = (c & 7) * 32 + (c >> 3);
    auto t0 = [&]() {
        u32x4 g;
#pragma unroll
        for (int j = 0; j < 4; ++j) g[j] = scale2(xv[j], dn);
        __builtin_amdgcn_raw_buffer_store_b128(g, mkrs(gtab, (u32)N * 128u), ro, 0, BA);
    };
    if (e0 < eend) {
        u32 bin[4], en[4], r[4];
#pragma unroll
        for (int j = 0; j < 4; ++j) {
            const u32 d = d4[j];
            bin[j] = (u32)__umul24(d, DIVM) >> 24;
            en[j] = (d - (u32)__umul24(bin[j], NPB)) << 24;
            if (j > 0 && e0 + j >= eend) bin[j] = NWG;
            r[j] = atomicAdd(&cnt[bin[j]], 1u);
        }
#pragma unroll
        for (int j = 0; j < 4; ++j) en[j] |= ((u32)__umul24(s4[j], DIVM) & 0xc00000u) | (s4[j] << 7);
#pragma unroll
        for (int j = 0; j < 4; ++j) cell[r[j] < CELL && bin[j] < NWG ? bin[j] * CELL + r[j] : NWG * CELL + OVH] = en[j];
        if (max(max(r[0], r[1]), max(r[2], r[3])) >= CELL) {
#pragma unroll 1
            for (int j = 0; j < 4; ++j) {
                const u32 rj = j == 0 ? r[0] : j == 1 ? r[1] : j == 2 ? r[2] : r[3];
                const u32 bj = j == 0 ? bin[0] : j == 1 ? bin[1] : j == 2 ? bin[2] : bin[3];
                const u32 ej = j == 0 ? en[0] : j == 1 ? en[1] : j == 2 ? en[2] : en[3];
                if (rj >= CELL && bj < NWG) {
                    const u32 o = atomicAdd(&ocnt, 1u);
                    if (o < OVH) cell[NWG * CELL + o] = (bj << 24) | (ej >> 7);
                    else ovf[(size_t)pos * chunk + o - OVH] = (bj << 24) | (ej >> 7);
                }
            }
        }
    }
    t0();
    code_done(cpv);
    STB(2);
    __syncthreads();
    STB(3);
#pragma unroll
    for (int h = 0; h < 2; ++h) {
        const u32 i = t + h * 1024, bin = (u32)__umul24(i, 10923u) >> 16;
        if (i < NWG * CELL / 4)
            __builtin_amdgcn_raw_buffer_store_b128(cell4[i], mkrs(cells + pos * (CELL / 4), (NWG * NWG - pos) * CELL * 4u),
                                                   (u32)__umul24(bin, (NWG - 1) * CELL * 4u) + i * 16u, 0, CA);
    }
    if (t >= 1008) ovh[((t + pos) & 15) * NWG + pos] = t == 1023 ? max(ocnt, (u32)OVH) - OVH + OVL : cell[NWG * CELL + t - 1008];
    STB(4);
    STB(5);
}

__device__ __forceinline__ int tokidx(int sl, int k, int piece) { return (sl * 8 + k) * 8 + (piece ^ ((k & 4) | ((sl & 1) << 1) | ((k >> 1) & 1))); }
__device__ __forceinline__ void bar() { asm volatile("s_waitcnt lgkmcnt(0)\n\ts_barrier" ::: "memory"); }
__device__ __forceinline__ void wsync() { asm volatile("" ::: "memory"); }
__device__ __forceinline__ u32x2 ldtr(u32 a) {
    typedef short v4s __attribute__((ext_vector_type(4)));
    return __builtin_bit_cast(u32x2, __builtin_amdgcn_ds_read_tr16_b64_v4i16((__attribute__((address_space(3))) v4s*)a));
}
template <typename T> __device__ __forceinline__ T ldl(u32 a) { return *(const __attribute__((address_space(3))) T*)a; }
template <typename T> __device__ __forceinline__ void stl(u32 a, T v) { *(__attribute__((address_space(3))) T*)a = v; }
__device__ __forceinline__ float swap16(float v, float& o) {
    const u32 a = __float_as_uint(v);
    auto q = __builtin_amdgcn_permlane16_swap(a, a, false, false);
    o = __uint_as_float(q[1]);
    return __uint_as_float(q[0]);
}
#define MM32(A, B, C) __builtin_amdgcn_mfma_f32_16x16x32_bf16(__builtin_bit_cast(bf8, A), __builtin_bit_cast(bf8, B), C, 0, 0, 0)
#define MM16(A, B, C) __builtin_amdgcn_mfma_f32_16x16x16bf16_1k(__builtin_bit_cast(s16x4, A), __builtin_bit_cast(s16x4, B), C, 0, 0, 0)
constexpr u32 SK7 = (7u << 7) | (4u << 4) | (2u << 3);
constexpr u32 FILL0 = 0x40000000u;

#define ORD 0
constexpr int ORD_P[16] = {0, 1, 2, 3, 0, 1, 2, 0, 0, 1, 1, 2, 2, 3, 3, 3};
constexpr int ORD_J[16] = {0, 0, 0, 0, 1, 1, 1, 2, 3, 2, 3, 2, 3, 1, 2, 3};

constexpr u32 packord(const int (&o)[16]) { u32 r = 0; for (int i = 0; i < 16; ++i) r |= (u32)o[i] << (2 * i); return r; }
constexpr int lastblk(int p) { int r = 0; for (int i = 0; i < 16; ++i) if (ORD_P[i] == p) r = i; return r; }
constexpr u32 ORD_PW = packord(ORD_P), ORD_JW = packord(ORD_J);

struct __attribute__((aligned(1024))) Shared {
    u32x4 pro[256 + 128 + OVCAP / 2 + 64];
    u32 misc[4];
    u32 wslot[NGW * 8];
    u32 tflag[NPB];
    u32 pinfo[NGW * 4];
    u32 npos[NPB + 30];
    u32 slotdl[NPB];
    u32 prog[16];
    u16 disl[NPB + 2];
    __attribute__((aligned(1024))) u32x4 tok[NPB * 64];
    u32x4 list[16 * MAXB * 8 > NPB * 16 ? 16 * MAXB * 8 : NPB * 16];
    u32x4 frag[2 * 8 * 64 + 2 * 64];
};

__global__ void __launch_bounds__(1024, 4)
k_main(const u32x4* __restrict__ cells, const u32* __restrict__ ovf, const u32* __restrict__ ovh, int chunk, int N,
       char* __restrict__ gtab, const u16* __restrict__ x, const u16* __restrict__ dis,
       const u16* __restrict__ wq, const u16* __restrict__ wk, const u16* __restrict__ wv, const u16* __restrict__ wo,
       const u16* __restrict__ wr, u16* __restrict__ out
       ) {
    __shared__ Shared sm;
    u32x4* const tok = sm.tok; u32x4* const list = sm.list; u32x4* const frag = sm.frag; u32x4* const pro = sm.pro;
    u32* const pinfo = sm.pinfo; u32* const npos = sm.npos; u32* const wslot = sm.wslot; u32* const slotdl = sm.slotdl;
    u32* const tflag = sm.tflag; u16* const disl = sm.disl; u32* const prog = sm.prog; u32* const misc = sm.misc;
    const u32 STG = ldsaddr(&sm) + (u32)offsetof(Shared, tok) + 512u;
    u32x4* const ktab4 = pro;
    u32x4* const cnt4 = pro + 256;
    u32x2* const stage = (u32x2*)(pro + 256 + 128);
    u32x2* const srt = (u32x2*)(pro + 256 + 128 + OVCAP / 2);
    const int b = blockIdx.x, t = threadIdx.x, lane = t & 63;
    const int w = __builtin_amdgcn_readfirstlane(t >> 6);
    const u32 TB = (u32)N * 128u;
#define STP(i)
    STP(0);
    asm volatile("KM_begin:");
    u32x4 ce0 = cells[(size_t)b * (NWG * CELL / 4) + t];
    u32x2 ce1 = ((const u32x2*)(cells + (size_t)b * (NWG * CELL / 4) + 1024))[t];
    u32x4 oh = ((const u32x4*)ovh)[t];
    const int node = b * NPB + (t >> 3);
    const bool own = t < NPB * 8 && node < N;
    const u32 ro = own ? (u32)node * 128u + (t & 7) * 16u : PADOFF;
    const u32x4 sent = {SENT, SENT, SENT, SENT};
    const u32 dv = __builtin_amdgcn_raw_buffer_load_b16(mkrs(dis, (u32)N * 2u), t < NPB ? (u32)(b * NPB + t) * 2u : PADOFF, 0, 0);
    auto fill = [&](int k) {
        const __amdgpu_buffer_rsrc_t rs = mkrs(gtab + (size_t)k * TS, TB);
        u32x4 sv = sent;
        asm volatile("" : "+v"(sv));
#pragma unroll
        for (int i = 0; i < (NPB * 8 + 63) / 64; ++i)
            __builtin_amdgcn_raw_buffer_store_b128(sv, rs, i * 64 + lane < NPB * 8 ? (u32)b * (NPB * 128u) + lane * 16u : PADOFF, i * 1024, FA);
        asm volatile("s_nop 1" :: "v"(sv));
    };
    u32 cpv[CPN];
    code_prefetch(lane, w == 14, cpv);
    constexpr int HF = HOPS - K0;
    auto fill0 = [&]() {
#pragma unroll
        for (int q = HF; q < HOPS; ++q) __builtin_amdgcn_raw_buffer_store_b128(sent, mkrs(gtab + (size_t)q * TS, TB), ro, 0, FA);
    };
    if (K0P == 0) fill0();
    {
        const u32x4 pad = {PADE, PADE, PADE, PADE};
        tok[(t >> 5) * 64 + 32 + (t & 31)] = pad;
        tok[(t >> 5) * 64 + 32 * 64 + 32 + (t & 31)] = pad;
        list[w * (MAXB * 8) + lane] = (u32x4){8, 8, 8, 8};
        if (t < NPB + 2) cnt4[t] = (u32x4){0, 0, 0, 0};
        if (t < 4) misc[t] = 0;
        if (t < 16) prog[t] = t < NGW ? 0u : FILL0 + KB + 1;
        if (t < NPB) tflag[t] = 0;
    }
    const int wl = min(w, NGW - 1);
    const u32 oq = (ORD_PW >> (2 * (lane & 15))) & 3u;
    u32 j8 = ((ORD_JW >> (2 * (lane & 15))) & 3u) * 8u;
    u32 a_pi = ldsaddr(pinfo + wl * 4 + oq), a_ws = ldsaddr(wslot + wl * 8 + (lane >> 3)), a_np = ldsaddr(npos + (t >> 3));
    u32 vb = (0x1000000u << oq) + STG + (wl * 4 + oq) * 1024u - 8u;
    u32 lwa = ldsaddr(list + w * (MAXB * 8) + (lane >> 2) * 8) + (lane & 3) * 4;
    u32 vlist = ldsaddr(list + w * MAXB * 8 + (lane >> 3)), lo = (lane & 7) * 16;
    asm volatile("" : "+v"(j8), "+v"(a_pi), "+v"(a_ws), "+v"(a_np), "+v"(vb), "+v"(lwa), "+v"(vlist), "+v"(lo));
    bar();
    u32* cnt = (u32*)cnt4;
    u32 rk[7], bk[7], ce[7];
    u64 vm[7];
#pragma unroll
    for (int h = 0; h < 3; ++h) {
        if (h == 0) asm volatile("" : "+v"(ce0));
        if (h == 2) asm volatile("" : "+v"(ce1));
#pragma unroll
        for (int i = 2 * h; i < 2 * h + 2; ++i) {
            ce[i] = i < 4 ? ce0[i & 3] : ce1[i & 1];
            bk[i] = ldsaddr(&sm) + ((ce[i] >> 20) & 0xffcu);
            vm[i] = __builtin_amdgcn_ballot_w64((int)ce[i] >= 0);
        }
        asm volatile("s_mov_b64 exec, %4\n\tds_add_rtn_u32 %0, %2, %6 offset:4096\n\t"
                     "s_mov_b64 exec, %5\n\tds_add_rtn_u32 %1, %3, %6 offset:4096\n\t"
                     "s_mov_b64 exec, -1"
                     : "=&v"(rk[2 * h]), "=&v"(rk[2 * h + 1])
                     : "v"(bk[2 * h]), "v"(bk[2 * h + 1]), "s"(vm[2 * h]), "s"(vm[2 * h + 1]), "v"(1u) : "memory");
    }
    {
        asm volatile("" : "+v"(oh));
        const u32x4 od = oh - ((u32)b << 24);
        const u32 om = min(min(od[0], od[1]), min(od[2], od[3]));
        const u64 m0 = __builtin_amdgcn_ballot_w64(od[0] < OVL), m1 = __builtin_amdgcn_ballot_w64(od[1] < OVL);
        const u64 m2 = __builtin_amdgcn_ballot_w64(od[2] < OVL), m3 = __builtin_amdgcn_ballot_w64(od[3] < OVL);
        ce[6] = om << 7;
        bk[6] = ldsaddr(&sm) + ((om >> 13) & 0xffcu);
        vm[6] = m0 | m1 | m2 | m3;
        asm volatile("s_mov_b64 exec, %2\n\tds_add_rtn_u32 %0, %1, %3 offset:4096\n\ts_mov_b64 exec, -1"
                     : "=&v"(rk[6]) : "v"(bk[6]), "s"(vm[6]), "v"(1u) : "memory");
        u64 more = ((m0 | m1) & (m2 | m3)) | (m0 & m1) | (m2 & m3);
        more |= __builtin_amdgcn_ballot_w64(((w & 2) ? ((w & 1) ? oh[0] : oh[3]) : ((w & 1) ? oh[2] : oh[1])) - OVL - 1u < 4096u);
        asm volatile("s_waitcnt lgkmcnt(0)" : "+v"(rk[0]), "+v"(rk[1]), "+v"(rk[2]), "+v"(rk[3]), "+v"(rk[4]), "+v"(rk[5]), "+v"(rk[6]) :: "memory");
        if (__builtin_expect(more != 0, 0)) {
            bool skip = true;
#pragma unroll 1
            for (int j = 0; j < 4; ++j) {
                const u32 hj = j == 0 ? oh[0] : j == 1 ? oh[1] : j == 2 ? oh[2] : oh[3];
                const bool ext = hj - OVL <= 4096u;
                const u32 n = ext ? hj - OVL : 1u;
#pragma unroll 1
                for (u32 i = 0; i < n; ++i) {
                    const u32 d = (ext ? ovf[(size_t)(lane * 4 + j) * chunk + i] : hj) - ((u32)b << 24);
                    if (d < OVL) {
                        if (skip && d == om) skip = false;
                        else {
                            const u32 r = atomicAdd(&cnt[d >> 15], 1u);
                            const u32 o = atomicAdd(&misc[0], 1u);
                            if (o < OVCAP) stage[o] = (u32x2){d << 7, r};
                        }
                    }
                }
            }
        }
    }
    if (t < NPB) disl[t] = (u16)dv;
    if (K0P == 1) fill0();
    const u32x4 xv = __builtin_amdgcn_raw_buffer_load_b128(mkrs(x, TB), ro, 0, 0);
    code_done(cpv);
    STP(1);
    bar();
    if (w == 15) {
#pragma unroll
        for (int q = KB + 1; q <= KB + KP1 && q < HF; ++q) fill(q);
    }
    if (w < 4) {
        const int np = 25 - (w & 1), S = 2 * NGW - np, dl = partbase(w) + lane;
        const bool val = lane < np;
        const u32x4 c = cnt4[val ? dl : NPB];
        const u32 a0 = c[0], a1 = a0 + c[1], a2 = a1 + c[2], deg = a2 + c[3];
        const u32 key = val ? (min(deg, 0xffffu) << 8) | (255u - lane) : 0u;
        u32 rank = 0;
#pragma unroll
        for (int i = 0; i < 25; ++i) rank += (u32)__builtin_amdgcn_readlane(key, i) > key;
        if (val) {
            const u32 slot = partbase(w) + rank;
            srt[w * 32 + rank] = (u32x2){(a0 & 255u) | ((a1 & 255u) << 8) | ((a2 & 255u) << 16) | (deg << 24), (u32)dl | (slot << 8)};
            const bool self = rank < (u32)S;
            const u32 j = self ? rank + (NGW - S) : (rank - S) >> 1;
            const u32 wg = (w == 0 || w == 3) ? j : NGW - 1 - j;
            const u32 sh = self ? 5u : 6u;
            const u32 x0 = (STG + (wg * 4 + w) * 1024u + (self ? 0u : ((rank - S) & 1u) * 4u)) * 8u + sh;
            ktab4[dl] = (u32x4){x0, x0 + (a0 << sh), x0 + (a1 << sh), x0 + (a2 << sh)};
            if (deg > (self ? 2u * SCAP : (u32)SCAP)) misc[1] = 1;
            npos[dl] = slot;
            slotdl[slot] = dl;
        }
        wsync();
        if (lane < NGW) {
            const bool self = lane >= NGW - S;
            const int ia = self ? lane - (NGW - S) : S + 2 * lane;
            const u32x2 A = srt[w * 32 + ia], B = srt[w * 32 + ia + !self];
            const u32 L = ((A[0] >> 24) + self) >> self, db = (B[0] >> 24) >> self;
            u32 pi = L << 24;
#pragma unroll
            for (int j = 0; j < 3; ++j) {
                const u32 aj = (((A[0] >> (8 * j)) & 255u) + self) >> self, bj = ((B[0] >> (8 * j)) & 255u) >> self;
                pi |= min(aj, bj == db ? L : bj) << (8 * j);
            }
            const int wg = (w == 0 || w == 3) ? lane : NGW - 1 - lane;
            pinfo[wg * 4 + w] = pi;
            *(u32x2*)(wslot + wg * 8 + w * 2) = (u32x2){A[1] | (self ? 0x10000u : 0u), B[1] | (self ? 0x30000u : 0u)};
        }
    }
    bar();
    STP(14);
    const u32 pi = ldl<u32>(a_pi);
    const u32 ws = ldl<u32>(a_ws);
    const u32x2 m01 = *(const u32x2*)misc;
    u32 kt[7];
#pragma unroll
    for (int i = 0; i < 7; ++i) kt[i] = ldl<u32>(bk[i]);
    const u32 myslot = ldl<u32>(a_np);
    const u32 ulo = ((pi << 8) >> j8) & 255u, len = ((pi >> j8) & 255u) - ulo;
    const u32 send = scan16(len);
    {
        const u32 v = vb + (ulo + len - send) * 8u;
        const u32 tail = STG + 60 * 1024u - (send + 1u) * 8u;
        const u32 jump = v + dppu<0x111, 15>(0u - v);
        u32* jw = (u32*)(list + w * (MAXB * 8));
        if (lane < 16) atomicAdd(&jw[(send - len) & 255u], jump);
        if (lane == 15) atomicAdd(&jw[send & 255u], tail - v);
    }
    wsync();
    u32x4 sa = list[w * (MAXB * 8) + lane];
    const u32 dn16 = disl[ws & 127u];
    const int total = __builtin_amdgcn_readlane(send, 15);
    if (__builtin_expect(__builtin_amdgcn_readfirstlane(m01[1] | (m01[0] > (u32)OVCAP)) == 0, 1)) {
#pragma unroll
        for (int i = 0; i < 7; ++i) kt[i] = ((rk[i] << (kt[i] & 31u)) + kt[i]) >> 3;
#pragma unroll
        for (int h = 0; h < 2; ++h)
            asm volatile("s_mov_b64 exec, %6\n\tds_write_b32 %0, %3\n\ts_mov_b64 exec, %7\n\tds_write_b32 %1, %4\n\t"
                         "s_mov_b64 exec, %8\n\tds_write_b32 %2, %5\n\ts_mov_b64 exec, -1"
                         :: "v"(kt[3 * h]), "v"(kt[3 * h + 1]), "v"(kt[3 * h + 2]), "v"(ce[3 * h]), "v"(ce[3 * h + 1]), "v"(ce[3 * h + 2]),
                            "s"(vm[3 * h]), "s"(vm[3 * h + 1]), "s"(vm[3 * h + 2]) : "memory");
        asm volatile("s_mov_b64 exec, %2\n\tds_write_b32 %0, %1\n\ts_mov_b64 exec, -1" :: "v"(kt[6]), "v"(ce[6]), "s"(vm[6]) : "memory");
        if ((u32)t < m01[0]) { const u32x2 s = stage[t]; const u32 k = ((const u32*)ktab4)[s[0] >> 22]; stl<u32>(((s[1] << (k & 31u)) + k) >> 3, s[0]); }
        if (total > CAPU) misc[1] = 1;
    }
    if (t < NPB * 8) tok[tokidx(myslot, 0, t & 7)] = xv;
    sa[1] += sa[0]; sa[2] += sa[1]; sa[3] += sa[2];
    sa += scan64(sa[3]) - sa[3];
    u32x4 sb4 = sa & 0x1fff8u;
    asm volatile("" : "+v"(sa), "+v"(sb4));
    const int nb = max((total + 15) >> 4, 1);
    if (w == 15) {
#pragma unroll 1
        for (int q = KB + KP1 + 1; q <= KB + KP && q < HF; ++q) fill(q);
    }
    if (K0 && w < 15) asm volatile("s_waitcnt vmcnt(0)" ::: "memory");
    bar();
    STP(15);
    const u32 m1 = misc[1];
    if (lane < MAXB * 4) {
        u32x2 un[4];
#pragma unroll
        for (int i = 0; i < 4; ++i) un[i] = ldl<u32x2>(sb4[i]);
#pragma unroll
        for (int i = 0; i < 4; ++i) { stl<u32>(lwa + i * 32, un[i][0] | (sa[i] >> 24)); stl<u32>(lwa + i * 32 + 16, un[i][1] | (sa[i] >> 24)); }
    }
    const bool slow = __builtin_amdgcn_readfirstlane(m1 | (m01[0] > (u32)OVCAP)) != 0;
    STP(2);
    const int nloc = min(NPB, N - b * NPB);
    if (w == 15) {
        if (FPOS == 2) fill(2);
        if (FPOS) fill(3);
        asm volatile("s_waitcnt vmcnt(0)" ::: "memory");
        prog[15] = FILL0 + (KB + KP + 1 < HF ? KB + KP + 1 : HOPS - 1);
        wsync();
        fuse_weights<true>(wq, wk, frag, lane);
        fuse_weights<false>(wv, wo, frag + 512, lane);
        float sw = 0.f;
#pragma unroll
        for (int h = 0; h < 2; ++h) {
            const u32x4 wv4 = *(const u32x4*)(wr + 32 * h + 8 * (lane >> 4));
            u32x4 a2;
#pragma unroll
            for (int j = 0; j < 4; ++j) { sw += blo(wv4[j]) + bhi(wv4[j]); a2[j] = (lane & 3) == 0 ? 0x3c803c80u : (lane & 3) == 1 ? wv4[j] : 0u; }
            frag[1024 + h * 64 + lane] = a2;
        }
        misc[2] = __float_as_uint(xsum1632(sw));
#pragma unroll 1
        for (int k = KB + KP + 1; k < HF; ++k) {
            for (;;) {
                wsync();
                if (slow || __builtin_amdgcn_readfirstlane(prog[0]) >= (u32)max(k - KB - KP - FD, 0) * 65000u) break;
                __builtin_amdgcn_s_sleep(16);
            }
            fill(k);
            asm volatile("s_waitcnt vmcnt(0)" ::: "memory");
            prog[15] = k + 1 < HF ? FILL0 + k + 1 : FILL0 + 64;
            wsync();
        }
        if (FD == 9 || KB + KP + 1 >= HF) { wsync(); prog[15] = FILL0 + 64; wsync(); }
    }
    if (!slow) {
        if (w < NGW) {
            const u32 nb128 = (u32)nb * 128u;
            const u64 gb = (u64)gtab;
            const u32x4 srd = {(u32)gb, (u32)(gb >> 32) & 0xffffu, TS * SRDT, 0x00020000u};
            u64 s_ret;
            u32 s_pb, s_cn, s_cb, s_sk, s_tmp, s_myp, budget = BUDGET;
            u32x16 p_ic, p_d;
            u32x4 p_ad;
            asm volatile(
                "ds_read_b128 v[68:71], %[vlist]\n"
                "ds_read_b128 v[76:79], %[vlist]\n"
                "s_mov_b32 %[pb], 128\n"
                "s_cmp_eq_u32 %[pb], %[nb128]\n"
                "s_cselect_b32 %[pb], 0, %[pb]\n"
                "s_mov_b32 %[cn], %[pb]\n"
                "v_add_u32 v97, %[pb], %[vlist]\n"
                "ds_read_b128 v[72:75], v97\n"
                "ds_read_b128 v[80:83], v97\n"
                "s_waitcnt lgkmcnt(3)\n"
                "v_and_or_b32 v108, v68, %[msk], %[lo]\n"
                "buffer_load_dwordx4 v[112:115], v108, %[srd], 0 offen sc1\n"
                "v_and_or_b32 v109, v69, %[msk], %[lo]\n"
                "buffer_load_dwordx4 v[116:119], v109, %[srd], 0 offen sc1\n"
                "v_and_or_b32 v110, v70, %[msk], %[lo]\n"
                "buffer_load_dwordx4 v[120:123], v110, %[srd], 0 offen sc1\n"
                "v_and_or_b32 v111, v71, %[msk], %[lo]\n"
                "buffer_load_dwordx4 v[124:127], v111, %[srd], 0 offen sc1\n"
                "s_waitcnt lgkmcnt(0)\n"
                : [pb] "=&s"(s_pb), [cn] "=&s"(s_cn), "=&{v[68:83]}"(p_ic), "=&{v[108:111]}"(p_ad), "=&{v[112:127]}"(p_d)
                : [srd] "s"(srd), [nb128] "s"(nb128), [msk] "s"(0x803fff80u), [vlist] "v"(vlist), [lo] "v"(lo)
                : "memory", "scc", "v97");
            u32 wz = ws, sz = send, lz = lane, dz = dn16;
            asm volatile("" : "+v"(wz), "+v"(sz), "+v"(lz), "+v"(dz));
            const u32 mydl = wz & 255u, mysl = (wz >> 8) & 255u;
            const float dn = __uint_as_float(dz << 16);
            int es[4];
#pragma unroll
            for (int q = 0; q < 4; ++q) es[q] = max((int)__builtin_amdgcn_readlane(sz, lastblk(q)) - 1, 0) >> 2;
            const int elane = lz < 32 ? (lz < 16 ? es[0] : es[1]) : (lz < 48 ? es[2] : es[3]);
            const u32 vflag = ldsaddr(tflag + mysl);
            const u32 soff = (int)mydl < nloc && !(wz & 0x20000u) ? (u32)(b * NPB + (int)mydl) * 128u + lo : PADOFF;
            const u64 smask = __builtin_amdgcn_ballot_w64((wz & 0x10000u) != 0);
            const u32 sh = (lz & 15) >> 2;
            const u32 oh0 = (lz & 3) == 0 ? 0x3f80u : (lz & 3) == 1 ? 0x3f800000u : 0u;
            const u32 oh1 = (lz & 3) == 2 ? 0x3f80u : (lz & 3) == 3 ? 0x3f800000u : 0u;
            const u32 vtok = ldsaddr(tok + tokidx(mysl, 0, lz & 7));
            u64 emask = 0;
#pragma unroll
            for (int q = 0; q < 4; ++q) emask |= 1ull << es[q];
            u32 incv = (u32)(65536.f * __builtin_amdgcn_rcpf((float)nb) + 1.f);
            asm volatile("" : "+v"(incv));
            const u32 inc = __builtin_amdgcn_readfirstlane(incv);
            const u32 vprog = ldsaddr(prog + (lz & 15)), vmine = ldsaddr(prog + w);
            asm volatile(
                "v_mov_b32 v84, 0\n"
                "v_mov_b32 v85, 0\n"
                "v_mov_b32 v86, 0\n"
                "v_mov_b32 v87, 0\n"
                "v_mov_b32 v88, 0\n"
                "v_mov_b32 v89, 0\n"
                "v_mov_b32 v90, 0\n"
                "v_mov_b32 v91, 0\n"
                "s_mov_b32 %[myp], 0\n"
                "ds_read_b32 v99, %[vprog]\n"
                "v_mov_b32 v95, %[lo]\n"
                "s_mov_b32 %[cb], 0\n"
                "s_mov_b32 %[sk], 128\n"
                "HL_loop:\n"
                "s_cmp_eq_u32 %[pb], 0\n"
                "s_cselect_b32 %[tmp], %[tb], 0\n"
                "v_add_u32 v95, %[tmp], v95\n"
                "s_add_u32 %[pb], %[pb], 128\n"
                "s_cmp_eq_u32 %[pb], %[nb128]\n"
                "s_cselect_b32 %[pb], 0, %[pb]\n"
                "v_add_u32 v97, %[pb], %[vlist]\n"
                "s_waitcnt lgkmcnt(0)\n"
                "ds_read_b128 v[68:71], v97\n"
                "v_bfe_i32 v94, v76, %[sh], 1\n"
                "v_and_b32 v92, %[oh0], v94\n"
                "v_and_b32 v93, %[oh1], v94\n"
                "s_waitcnt vmcnt(3)\n"
                "HL_chk0:\n"
                "v_max3_u32 v96, v112, v113, v114\n"
                "v_max_u32 v96, v96, v115\n"
                "v_cmp_eq_u32 vcc, -1, v96\n"
                "s_cbranch_vccnz HL_retry0\n"
                "HL_back0:\n"
                "v_and_or_b32 v108, v72, %[msk], v95\n"
                "v_mfma_f32_16x16x16_bf16 v[84:87], v[92:93], v[112:113], v[84:87]\n"
                "v_mfma_f32_16x16x16_bf16 v[88:91], v[92:93], v[114:115], v[88:91]\n"
                "buffer_load_dwordx4 v[112:115], v108, %[srd], 0 offen sc1\n"
                "s_bitcmp1_b64 %[emask], %[cb]\n"
                "s_cbranch_scc1 HL_epi0\n"
                "HL_eback0:\n"
                "s_add_u32 %[cb], %[cb], 1\n"
                "v_bfe_i32 v94, v77, %[sh], 1\n"
                "v_and_b32 v92, %[oh0], v94\n"
                "v_and_b32 v93, %[oh1], v94\n"
                "s_waitcnt vmcnt(3)\n"
                "HL_chk1:\n"
                "v_max3_u32 v96, v116, v117, v118\n"
                "v_max_u32 v96, v96, v119\n"
                "v_cmp_eq_u32 vcc, -1, v96\n"
                "s_cbranch_vccnz HL_retry1\n"
                "HL_back1:\n"
                "v_and_or_b32 v109, v73, %[msk], v95\n"
                "v_mfma_f32_16x16x16_bf16 v[84:87], v[92:93], v[116:117], v[84:87]\n"
                "v_mfma_f32_16x16x16_bf16 v[88:91], v[92:93], v[118:119], v[88:91]\n"
                "buffer_load_dwordx4 v[116:119], v109, %[srd], 0 offen sc1\n"
                "s_bitcmp1_b64 %[emask], %[cb]\n"
                "s_cbranch_scc1 HL_epi1\n"
                "HL_eback1:\n"
                "s_add_u32 %[cb], %[cb], 1\n"
                "v_bfe_i32 v94, v78, %[sh], 1\n"
                "v_and_b32 v92, %[oh0], v94\n"
                "v_and_b32 v93, %[oh1], v94\n"
                "s_waitcnt vmcnt(3)\n"
                "HL_chk2:\n"
                "v_max3_u32 v96, v120, v121, v122\n"
                "v_max_u32 v96, v96, v123\n"
                "v_cmp_eq_u32 vcc, -1, v96\n"
                "s_cbranch_vccnz HL_retry2\n"
                "HL_back2:\n"
                "v_and_or_b32 v110, v74, %[msk], v95\n"
                "v_mfma_f32_16x16x16_bf16 v[84:87], v[92:93], v[120:121], v[84:87]\n"
                "v_mfma_f32_16x16x16_bf16 v[88:91], v[92:93], v[122:123], v[88:91]\n"
                "buffer_load_dwordx4 v[120:123], v110, %[srd], 0 offen sc1\n"
                "s_bitcmp1_b64 %[emask], %[cb]\n"
                "s_cbranch_scc1 HL_epi2\n"
                "HL_eback2:\n"
                "s_add_u32 %[cb], %[cb], 1\n"
                "v_bfe_i32 v94, v79, %[sh], 1\n"
                "v_and_b32 v92, %[oh0], v94\n"
                "v_and_b32 v93, %[oh1], v94\n"
                "s_waitcnt vmcnt(3)\n"
                "HL_chk3:\n"
                "v_max3_u32 v96, v124, v125, v126\n"
                "v_max_u32 v96, v96, v127\n"
                "v_cmp_eq_u32 vcc, -1, v96\n"
                "s_cbranch_vccnz HL_retry3\n"
                "HL_back3:\n"
                "v_and_or_b32 v111, v75, %[msk], v95\n"
                "v_mfma_f32_16x16x16_bf16 v[84:87], v[92:93], v[124:125], v[84:87]\n"
                "v_mfma_f32_16x16x16_bf16 v[88:91], v[92:93], v[126:127], v[88:91]\n"
                "buffer_load_dwordx4 v[124:127], v111, %[srd], 0 offen sc1\n"
                "s_bitcmp1_b64 %[emask], %[cb]\n"
                "s_cbranch_scc1 HL_epi3\n"
                "HL_eback3:\n"
                "s_add_u32 %[cb], %[cb], 1\n"
                "s_cmp_eq_u32 %[cn], 0\n"
                "s_cbranch_scc1 HL_wrap0\n"
                "HL_wback0:\n"
                "s_add_u32 %[cn], %[cn], 128\n"
                "s_cmp_eq_u32 %[cn], %[nb128]\n"
                "s_cselect_b32 %[cn], 0, %[cn]\n"
                "v_add_u32 v97, %[cn], %[vlist]\n"
                "s_waitcnt lgkmcnt(0)\n"
                "s_add_u32 %[myp], %[myp], %[inc]\n"
                "v_cmp_gt_u32 vcc, %[myp], v99\n"
                "v_mov_b32 v96, %[myp]\n"
                "ds_read_b128 v[76:79], v97\n"
                "ds_write_b32 %[vmine], v96\n"
                "s_bcnt1_i32_b32 %[tmp], vcc_lo\n"
                "s_setprio 3\n"
                "s_cmp_lt_u32 %[tmp], 8\n"
                "s_cbranch_scc1 HL_pd0\n"
                "s_setprio 2\n"
                "s_cmp_lt_u32 %[tmp], 16\n"
                "s_cbranch_scc1 HL_pd0\n"
                "s_setprio 1\n"
                "s_cmp_lt_u32 %[tmp], 22\n"
                "s_cbranch_scc1 HL_pd0\n"
                "s_setprio 0\n"
                "HL_pd0:\n"
                "s_sub_i32 %[tmp], %[myp], 20000\n"
                "HL_gc0:\n"
                "v_cmp_gt_i32 vcc, %[tmp], v99\n"
                "ds_read_b32 v99, %[vprog]\n"
                "s_cbranch_vccnz HL_gw0\n"
                "s_cmp_eq_u32 %[pb], 0\n"
                "s_cselect_b32 %[tmp], %[tb], 0\n"
                "v_add_u32 v95, %[tmp], v95\n"
                "s_add_u32 %[pb], %[pb], 128\n"
                "s_cmp_eq_u32 %[pb], %[nb128]\n"
                "s_cselect_b32 %[pb], 0, %[pb]\n"
                "v_add_u32 v97, %[pb], %[vlist]\n"
                "s_waitcnt lgkmcnt(0)\n"
                "ds_read_b128 v[72:75], v97\n"
                "v_bfe_i32 v94, v80, %[sh], 1\n"
                "v_and_b32 v92, %[oh0], v94\n"
                "v_and_b32 v93, %[oh1], v94\n"
                "s_waitcnt vmcnt(3)\n"
                "HL_chk4:\n"
                "v_max3_u32 v96, v112, v113, v114\n"
                "v_max_u32 v96, v96, v115\n"
                "v_cmp_eq_u32 vcc, -1, v96\n"
                "s_cbranch_vccnz HL_retry4\n"
                "HL_back4:\n"
                "v_and_or_b32 v108, v68, %[msk], v95\n"
                "v_mfma_f32_16x16x16_bf16 v[84:87], v[92:93], v[112:113], v[84:87]\n"
                "v_mfma_f32_16x16x16_bf16 v[88:91], v[92:93], v[114:115], v[88:91]\n"
                "buffer_load_dwordx4 v[112:115], v108, %[srd], 0 offen sc1\n"
                "s_bitcmp1_b64 %[emask], %[cb]\n"
                "s_cbranch_scc1 HL_epi4\n"
                "HL_eback4:\n"
                "s_add_u32 %[cb], %[cb], 1\n"
                "v_bfe_i32 v94, v81, %[sh], 1\n"
                "v_and_b32 v92, %[oh0], v94\n"
                "v_and_b32 v93, %[oh1], v94\n"
                "s_waitcnt vmcnt(3)\n"
                "HL_chk5:\n"
                "v_max3_u32 v96, v116, v117, v118\n"
                "v_max_u32 v96, v96, v119\n"
                "v_cmp_eq_u32 vcc, -1, v96\n"
                "s_cbranch_vccnz HL_retry5\n"
                "HL_back5:\n"
                "v_and_or_b32 v109, v69, %[msk], v95\n"
                "v_mfma_f32_16x16x16_bf16 v[84:87], v[92:93], v[116:117], v[84:87]\n"
                "v_mfma_f32_16x16x16_bf16 v[88:91], v[92:93], v[118:119], v[88:91]\n"
                "buffer_load_dwordx4 v[116:119], v109, %[srd], 0 offen sc1\n"
                "s_bitcmp1_b64 %[emask], %[cb]\n"
                "s_cbranch_scc1 HL_epi5\n"
                "HL_eback5:\n"
                "s_add_u32 %[cb], %[cb], 1\n"
                "v_bfe_i32 v94, v82, %[sh], 1\n"
                "v_and_b32 v92, %[oh0], v94\n"
                "v_and_b32 v93, %[oh1], v94\n"
                "s_waitcnt vmcnt(3)\n"
                "HL_chk6:\n"
                "v_max3_u32 v96, v120, v121, v122\n"
                "v_max_u32 v96, v96, v123\n"
                "v_cmp_eq_u32 vcc, -1, v96\n"
                "s_cbranch_vccnz HL_retry6\n"
                "HL_back6:\n"
                "v_and_or_b32 v110, v70, %[msk], v95\n"
                "v_mfma_f32_16x16x16_bf16 v[84:87], v[92:93], v[120:121], v[84:87]\n"
                "v_mfma_f32_16x16x16_bf16 v[88:91], v[92:93], v[122:123], v[88:91]\n"
                "buffer_load_dwordx4 v[120:123], v110, %[srd], 0 offen sc1\n"
                "s_bitcmp1_b64 %[emask], %[cb]\n"
                "s_cbranch_scc1 HL_epi6\n"
                "HL_eback6:\n"
                "s_add_u32 %[cb], %[cb], 1\n"
                "v_bfe_i32 v94, v83, %[sh], 1\n"
                "v_and_b32 v92, %[oh0], v94\n"
                "v_and_b32 v93, %[oh1], v94\n"
                "s_waitcnt vmcnt(3)\n"
                "HL_chk7:\n"
                "v_max3_u32 v96, v124, v125, v126\n"
                "v_max_u32 v96, v96, v127\n"
                "v_cmp_eq_u32 vcc, -1, v96\n"
                "s_cbranch_vccnz HL_retry7\n"
                "HL_back7:\n"
                "v_and_or_b32 v111, v71, %[msk], v95\n"
                "v_mfma_f32_16x16x16_bf16 v[84:87], v[92:93], v[124:125], v[84:87]\n"
                "v_mfma_f32_16x16x16_bf16 v[88:91], v[92:93], v[126:127], v[88:91]\n"
                "buffer_load_dwordx4 v[124:127], v111, %[srd], 0 offen sc1\n"
                "s_bitcmp1_b64 %[emask], %[cb]\n"
                "s_cbranch_scc1 HL_epi7\n"
                "HL_eback7:\n"
                "s_add_u32 %[cb], %[cb], 1\n"
                "s_cmp_eq_u32 %[cn], 0\n"
                "s_cbranch_scc1 HL_wrap1\n"
                "HL_wback1:\n"
                "s_add_u32 %[cn], %[cn], 128\n"
                "s_cmp_eq_u32 %[cn], %[nb128]\n"
                "s_cselect_b32 %[cn], 0, %[cn]\n"
                "v_add_u32 v97, %[cn], %[vlist]\n"
                "s_waitcnt lgkmcnt(0)\n"
                "s_add_u32 %[myp], %[myp], %[inc]\n"
                "v_cmp_gt_u32 vcc, %[myp], v99\n"
                "v_mov_b32 v96, %[myp]\n"
                "ds_read_b128 v[80:83], v97\n"
                "ds_write_b32 %[vmine], v96\n"
                "s_bcnt1_i32_b32 %[tmp], vcc_lo\n"
                "s_setprio 3\n"
                "s_cmp_lt_u32 %[tmp], 8\n"
                "s_cbranch_scc1 HL_pd1\n"
                "s_setprio 2\n"
                "s_cmp_lt_u32 %[tmp], 16\n"
                "s_cbranch_scc1 HL_pd1\n"
                "s_setprio 1\n"
                "s_cmp_lt_u32 %[tmp], 22\n"
                "s_cbranch_scc1 HL_pd1\n"
                "s_setprio 0\n"
                "HL_pd1:\n"
                "s_sub_i32 %[tmp], %[myp], 20000\n"
                "HL_gc1:\n"
                "v_cmp_gt_i32 vcc, %[tmp], v99\n"
                "ds_read_b32 v99, %[vprog]\n"
                "s_cbranch_vccnz HL_gw1\n"
                "s_branch HL_loop\n"
                "HL_gw0:\n"
                "s_sleep 1\n"
                "s_waitcnt lgkmcnt(0)\n"
                "s_branch HL_gc0\n"
                "HL_gw1:\n"
                "s_sleep 1\n"
                "s_waitcnt lgkmcnt(0)\n"
                "s_branch HL_gc1\n"
                "HL_retry0:\n"
                "s_call_b64 %[ret], HL_pr\n"
                "s_cbranch_scc0 HL_chk0\n"
                "s_branch HL_back0\n"
                "HL_retry1:\n"
                "s_call_b64 %[ret], HL_pr\n"
                "s_cbranch_scc0 HL_chk1\n"
                "s_branch HL_back1\n"
                "HL_retry2:\n"
                "s_call_b64 %[ret], HL_pr\n"
                "s_cbranch_scc0 HL_chk2\n"
                "s_branch HL_back2\n"
                "HL_retry3:\n"
                "s_call_b64 %[ret], HL_pr\n"
                "s_cbranch_scc0 HL_chk3\n"
                "s_branch HL_back3\n"
                "HL_retry4:\n"
                "s_call_b64 %[ret], HL_pr\n"
                "s_cbranch_scc0 HL_chk4\n"
                "s_branch HL_back4\n"
                "HL_retry5:\n"
                "s_call_b64 %[ret], HL_pr\n"
                "s_cbranch_scc0 HL_chk5\n"
                "s_branch HL_back5\n"
                "HL_retry6:\n"
                "s_call_b64 %[ret], HL_pr\n"
                "s_cbranch_scc0 HL_chk6\n"
                "s_branch HL_back6\n"
                "HL_retry7:\n"
                "s_call_b64 %[ret], HL_pr\n"
                "s_cbranch_scc0 HL_chk7\n"
                "s_branch HL_back7\n"
                "HL_pr:\n"
                "s_cmp_eq_u32 %[bud], 0\n"
                "s_cbranch_scc1 HL_prx\n"
                "s_sub_u32 %[bud], %[bud], 1\n"
                "s_sleep 8\n"
                "s_waitcnt vmcnt(0)\n"
                "v_max3_u32 v96, v112, v113, v114\n"
                "v_max_u32 v96, v96, v115\n"
                "v_cmp_eq_u32 vcc, -1, v96\n"
                "s_cbranch_vccz HL_ps0\n"
                "s_mov_b64 exec, vcc\n"
                "buffer_load_dwordx4 v[112:115], v108, %[srd], 0 offen sc1\n"
                "s_mov_b64 exec, -1\n"
                "HL_ps0:\n"
                "v_max3_u32 v96, v116, v117, v118\n"
                "v_max_u32 v96, v96, v119\n"
                "v_cmp_eq_u32 vcc, -1, v96\n"
                "s_cbranch_vccz HL_ps1\n"
                "s_mov_b64 exec, vcc\n"
                "buffer_load_dwordx4 v[116:119], v109, %[srd], 0 offen sc1\n"
                "s_mov_b64 exec, -1\n"
                "HL_ps1:\n"
                "v_max3_u32 v96, v120, v121, v122\n"
                "v_max_u32 v96, v96, v123\n"
                "v_cmp_eq_u32 vcc, -1, v96\n"
                "s_cbranch_vccz HL_ps2\n"
                "s_mov_b64 exec, vcc\n"
                "buffer_load_dwordx4 v[120:123], v110, %[srd], 0 offen sc1\n"
                "s_mov_b64 exec, -1\n"
                "HL_ps2:\n"
                "v_max3_u32 v96, v124, v125, v126\n"
                "v_max_u32 v96, v96, v127\n"
                "v_cmp_eq_u32 vcc, -1, v96\n"
                "s_cbranch_vccz HL_ps3\n"
                "s_mov_b64 exec, vcc\n"
                "buffer_load_dwordx4 v[124:127], v111, %[srd], 0 offen sc1\n"
                "s_mov_b64 exec, -1\n"
                "HL_ps3:\n"
                "s_waitcnt vmcnt(0)\n"
                "s_cmp_eq_u32 0, 1\n"
                "HL_prx:\n"
                "s_setpc_b64 %[ret]\n"
                "HL_epi0:\n"
                "s_call_b64 %[ret], HL_ep\n"
                "s_branch HL_eback0\n"
                "HL_epi1:\n"
                "s_call_b64 %[ret], HL_ep\n"
                "s_branch HL_eback1\n"
                "HL_epi2:\n"
                "s_call_b64 %[ret], HL_ep\n"
                "s_branch HL_eback2\n"
                "HL_epi3:\n"
                "s_call_b64 %[ret], HL_ep\n"
                "s_branch HL_eback3\n"
                "HL_epi4:\n"
                "s_call_b64 %[ret], HL_ep\n"
                "s_branch HL_eback4\n"
                "HL_epi5:\n"
                "s_call_b64 %[ret], HL_ep\n"
                "s_branch HL_eback5\n"
                "HL_epi6:\n"
                "s_call_b64 %[ret], HL_ep\n"
                "s_branch HL_eback6\n"
                "HL_epi7:\n"
                "s_call_b64 %[ret], HL_ep\n"
                "s_branch HL_eback7\n"
                "HL_ep:\n"
                "s_waitcnt lgkmcnt(0)\n"
                "v_readlane_b32 %[tmp], v99, 15\n"
                "s_lshr_b32 vcc_lo, %[sk], 7\n"
                "s_add_u32 vcc_hi, vcc_lo, 0x40000002\n"
                "s_cmp_ge_u32 %[tmp], vcc_hi\n"
                "s_cbranch_scc1 HL_fok\n"
                "s_sleep 4\n"
                "ds_read_b32 v99, %[vprog]\n"
                "s_branch HL_ep\n"
                "HL_fok:\n"
                "s_lshl_b32 %[tmp], vcc_lo, 22\n"
                "v_cmp_eq_u32 vcc, %[cb], %[elane]\n"
                "v_xor_b32 v97, %[sk], %[vtok]\n"
                "v_mov_b32 v96, %[sk]\n"
                "s_and_b64 exec, vcc, %[smask]\n"
                "s_nop 1\n"
                "v_add_f32_dpp v84, v84, v84 row_ror:8 row_mask:0xf bank_mask:0xf\n"
                "v_add_f32_dpp v85, v85, v85 row_ror:8 row_mask:0xf bank_mask:0xf\n"
                "v_add_f32_dpp v86, v86, v86 row_ror:8 row_mask:0xf bank_mask:0xf\n"
                "v_add_f32_dpp v87, v87, v87 row_ror:8 row_mask:0xf bank_mask:0xf\n"
                "v_add_f32_dpp v88, v88, v88 row_ror:8 row_mask:0xf bank_mask:0xf\n"
                "v_add_f32_dpp v89, v89, v89 row_ror:8 row_mask:0xf bank_mask:0xf\n"
                "v_add_f32_dpp v90, v90, v90 row_ror:8 row_mask:0xf bank_mask:0xf\n"
                "v_add_f32_dpp v91, v91, v91 row_ror:8 row_mask:0xf bank_mask:0xf\n"
                "s_mov_b64 exec, vcc\n"
                "v_mul_f32 v84, v84, %[dn]\n"
                "v_mul_f32 v85, v85, %[dn]\n"
                "v_mul_f32 v86, v86, %[dn]\n"
                "v_mul_f32 v87, v87, %[dn]\n"
                "v_mul_f32 v88, v88, %[dn]\n"
                "v_mul_f32 v89, v89, %[dn]\n"
                "v_mul_f32 v90, v90, %[dn]\n"
                "v_mul_f32 v91, v91, %[dn]\n"
                "v_cvt_pk_bf16_f32 v100, v84, v85\n"
                "v_cvt_pk_bf16_f32 v101, v86, v87\n"
                "v_cvt_pk_bf16_f32 v102, v88, v89\n"
                "v_cvt_pk_bf16_f32 v103, v90, v91\n"
                "v_lshlrev_b32 v84, 16, v100\n"
                "v_and_b32 v85, 0xffff0000, v100\n"
                "v_lshlrev_b32 v86, 16, v101\n"
                "v_and_b32 v87, 0xffff0000, v101\n"
                "v_lshlrev_b32 v88, 16, v102\n"
                "v_and_b32 v89, 0xffff0000, v102\n"
                "v_lshlrev_b32 v90, 16, v103\n"
                "v_and_b32 v91, 0xffff0000, v103\n"
                "v_mul_f32 v84, v84, %[dn]\n"
                "v_mul_f32 v85, v85, %[dn]\n"
                "v_mul_f32 v86, v86, %[dn]\n"
                "v_mul_f32 v87, v87, %[dn]\n"
                "v_mul_f32 v88, v88, %[dn]\n"
                "v_mul_f32 v89, v89, %[dn]\n"
                "v_mul_f32 v90, v90, %[dn]\n"
                "v_mul_f32 v91, v91, %[dn]\n"
                "v_cvt_pk_bf16_f32 v104, v84, v85\n"
                "v_cvt_pk_bf16_f32 v105, v86, v87\n"
                "v_cvt_pk_bf16_f32 v106, v88, v89\n"
                "v_cvt_pk_bf16_f32 v107, v90, v91\n"
                "v_min_u32 v104, -2, v104\n"
                "v_min_u32 v105, -2, v105\n"
                "v_min_u32 v106, -2, v106\n"
                "v_min_u32 v107, -2, v107\n"
                "buffer_store_dwordx4 v[104:107], %[soff], %[srd], %[tmp] offen sc1 nt\n"
                "ds_write_b128 v97, v[100:103]\n"
                "ds_write_b32 %[vflag], v96\n"
                "v_mov_b32 v84, 0\n"
                "v_mov_b32 v85, 0\n"
                "v_mov_b32 v86, 0\n"
                "v_mov_b32 v87, 0\n"
                "v_mov_b32 v88, 0\n"
                "v_mov_b32 v89, 0\n"
                "v_mov_b32 v90, 0\n"
                "v_mov_b32 v91, 0\n"
                "s_mov_b64 exec, -1\n"
                "s_setpc_b64 %[ret]\n"
                "HL_wrap0:\n"
                "s_mov_b32 %[cb], 0\n"
                "s_lshr_b32 %[tmp], %[sk], 7\n"
                "s_add_u32 %[tmp], %[tmp], 1\n"
                "s_cmp_eq_u32 %[tmp], 8\n"
                "s_cbranch_scc1 HL_done\n"
                "s_lshl_b32 %[sk], %[tmp], 7\n"
                "s_and_b32 vcc_lo, %[tmp], 4\n"
                "s_lshl_b32 vcc_lo, vcc_lo, 4\n"
                "s_or_b32 %[sk], %[sk], vcc_lo\n"
                "s_and_b32 vcc_lo, %[tmp], 2\n"
                "s_lshl_b32 vcc_lo, vcc_lo, 3\n"
                "s_or_b32 %[sk], %[sk], vcc_lo\n"
                "s_branch HL_wback0\n"
                "HL_wrap1:\n"
                "s_mov_b32 %[cb], 0\n"
                "s_lshr_b32 %[tmp], %[sk], 7\n"
                "s_add_u32 %[tmp], %[tmp], 1\n"
                "s_cmp_eq_u32 %[tmp], 8\n"
                "s_cbranch_scc1 HL_done\n"
                "s_lshl_b32 %[sk], %[tmp], 7\n"
                "s_and_b32 vcc_lo, %[tmp], 4\n"
                "s_lshl_b32 vcc_lo, vcc_lo, 4\n"
                "s_or_b32 %[sk], %[sk], vcc_lo\n"
                "s_and_b32 vcc_lo, %[tmp], 2\n"
                "s_lshl_b32 vcc_lo, vcc_lo, 3\n"
                "s_or_b32 %[sk], %[sk], vcc_lo\n"
                "s_branch HL_wback1\n"
                "HL_done:\n"
                "v_mov_b32 v96, 0x40000000\n"
                "ds_write_b32 %[vmine], v96\n"
                "s_setprio 0\n"
                "s_waitcnt vmcnt(0) lgkmcnt(0)\n"
                : "+{v[68:83]}"(p_ic), "+{v[108:111]}"(p_ad), "+{v[112:127]}"(p_d), [pb] "+s"(s_pb), [cn] "+s"(s_cn), [ret] "=&s"(s_ret), [cb] "=&s"(s_cb), [sk] "=&s"(s_sk), [tmp] "=&s"(s_tmp), [bud] "+s"(budget), [myp] "=&s"(s_myp)
                : [srd] "s"(srd), [tb] "s"(LTB), [nb128] "s"(nb128), [msk] "s"(0x803fff80u), [emask] "s"(emask), [smask] "s"(smask), [vlist] "v"(vlist), [vtok] "v"(vtok), [lo] "v"(lo), [sh] "v"(sh), [oh0] "v"(oh0), [oh1] "v"(oh1), [elane] "v"(elane), [dn] "v"(dn), [soff] "v"(soff), [inc] "s"(inc), [vprog] "v"(vprog), [vmine] "v"(vmine), [vflag] "v"(vflag)
                : "memory", "vcc", "scc", "v84", "v85", "v86", "v87", "v88", "v89", "v90", "v91", "v92", "v93", "v94", "v95", "v96", "v97", "v98", "v99", "v100", "v101", "v102", "v103", "v104", "v105", "v106", "v107");
        }
    } else {
        float* facc = (float*)list;
        __syncthreads();
        u32 tries = 0;
        const u32* cw = (const u32*)(cells + (size_t)b * (NWG * CELL / 4));
        const float d2 = bf2f(__builtin_amdgcn_raw_buffer_load_b16(mkrs(dis, (u32)N * 2u), own ? (u32)node * 2u : PADOFF, 0, 0));
        for (int k = 0; k < HOPS; ++k) {
            for (int i = t; i < NPB * 64; i += 1024) facc[i] = 0.f;
            __syncthreads();
            const __amdgpu_buffer_rsrc_t rs = mkrs(gtab + (size_t)k * TS, TB);
            auto add = [&](u32 e) {
                u32x4 r;
                for (;;) {
                    r = __builtin_amdgcn_raw_buffer_load_b128(rs, (e & 0x3fff80u) | lo, 0, 16);
                    if (max4(r) != SENT || tries >= BUDGET) break;
                    ++tries;
                    __builtin_amdgcn_s_sleep(8);
                }
                float* p = facc + ((e >> 24) & 127u) * 64 + (lane & 7) * 8;
#pragma unroll
                for (int j = 0; j < 4; ++j) { atomicAdd(p + 2 * j, blo(r[j])); atomicAdd(p + 2 * j + 1, bhi(r[j])); }
            };
            for (int i = t >> 3; i < NWG * CELL; i += 128) {
                const u32 e = cw[i];
                if ((int)e >= 0) add(e);
            }
            for (int i = t >> 3; i < NWG * 16; i += 128) {
                const u32 d = ovh[i] - ((u32)b << 24);
                if (d < OVL) add(d << 7);
            }
            for (int c = 0; c < NWG; ++c) {
                const u32 n = ovh[((OVH + c) & 15) * NWG + c] - OVL;
                for (u32 i = t >> 3; i < n; i += 128) {
                    const u32 d = ovf[(size_t)c * chunk + i] - ((u32)b << 24);
                    if (d < OVL) add(d << 7);
                }
            }
            __syncthreads();
            if (t < NPB * 8) {
                const float* p = facc + t * 8;
                const u32x4 h = {pk(p[0] * d2, p[1] * d2), pk(p[2] * d2, p[3] * d2), pk(p[4] * d2, p[5] * d2), pk(p[6] * d2, p[7] * d2)};
                const u32x4 g = {scale2(h[0], d2), scale2(h[1], d2), scale2(h[2], d2), scale2(h[3], d2)};
                __builtin_amdgcn_raw_buffer_store_b128(g, mkrs(gtab + (size_t)(k + 1) * TS, k + 1 < HOPS ? TB : 0u), ro, 0, 16);
                tok[tokidx(myslot, k + 1, t & 7)] = h;
            }
            __syncthreads();
        }
        if (t < NPB) tflag[t] = SK7;
        __syncthreads();
    }
    STP(10);
    {
        u32 acl = ldsaddr(&sm) + (u32)offsetof(Shared, misc) + 12u;
        asm volatile("" : "+v"(acl));
        u32 nx;
        auto claim = [&]() { asm volatile("s_mov_b64 exec, 1\n\tds_add_rtn_u32 %0, %1, %2\n\ts_mov_b64 exec, -1" : "=&v"(nx) : "v"(acl), "v"(1u) : "memory"); };
        auto claimed = [&]() {
            u32 i;
            asm volatile("s_waitcnt lgkmcnt(0)\n\tv_readfirstlane_b32 %0, %1" : "=s"(i) : "v"(nx) : "memory");
            return i;
        };
        claim();
        {
            const int r = lane & 15, g = lane >> 4;
            u32x4 Mf[8], A2[2];
#pragma unroll
            for (int i = 0; i < 8; ++i) Mf[i] = frag[i * 64 + lane];
            const u32 awf = ldsaddr(frag + 8 * 64 + lane);
            A2[0] = frag[1024 + lane]; A2[1] = frag[1024 + 64 + lane];
            const float sw = __uint_as_float(__builtin_amdgcn_readfirstlane(misc[2]));
            const bool valid = (g >> 1) == (r >> 3), diag = g == (r >> 2);
            const u32 id0 = !diag ? 0u : (r & 3) == 0 ? 0x3f80u : (r & 3) == 1 ? 0x3f800000u : 0u;
            const u32 id1 = !diag ? 0u : (r & 3) == 2 ? 0x3f80u : (r & 3) == 3 ? 0x3f800000u : 0u;
            const u32 ax = ldsaddr(tok + tokidx(r >> 3, r & 7, g));
            const u32 at = ldsaddr(tok + tokidx(lane >> 5, (lane >> 2) & 7, lane & 3));
            const u32 sb = ldsaddr(pro) + w * 512, afl = ldsaddr(tflag + (r >> 3));
            const f32x4 zero = {0, 0, 0, 0};
#pragma unroll 1
            for (u32 tile = claimed(); tile < (u32)(NPB / 2); tile = claimed()) {
                const u32 a0 = ax + tile * 2048, a1 = at + tile * 2048, af = afl + tile * 8;
                u32x4 x0, x1;
                for (;;) {
                    wsync();
                    const u32 fl = ldl<u32>(af);
                    wsync();
                    x0 = ldl<u32x4>(a0); x1 = ldl<u32x4>(a0 ^ 64);
                    wsync();
                    if (__builtin_expect(__builtin_amdgcn_ballot_w64(fl != SK7) == 0, 1)) break;
                    __builtin_amdgcn_s_sleep(2);
                }
                const u32 dl = ldl<u32>(af + (u32)(offsetof(Shared, slotdl) - offsetof(Shared, tflag)));
                const u32x2 xt0 = ldtr(a1), xt1 = ldtr(a1 + 8), xt2 = ldtr(a1 ^ 64), xt3 = ldtr((a1 ^ 64) + 8);
                f32x4 G = MM32(x0, x0, zero); G = MM32(x1, x1, G);
                f32x4 mu = MM32(A2[0], x0, zero); mu = MM32(A2[1], x1, mu);
                f32x4 U[4];
#pragma unroll
                for (int et = 0; et < 4; ++et) { U[et] = MM32(Mf[2 * et], x0, zero); U[et] = MM32(Mf[2 * et + 1], x1, U[et]); }
                if (diag) stl<f32x4>(sb + r * 16, G);
                wsync();
                const float q1 = ldl<float>(sb + r * 16 + (r & 3) * 4);
                const float rho = __builtin_amdgcn_rsqf(fmaxf(fmaf(q1, 1.f / 64, fmaf(-mu[0], mu[0], 1e-5f)), 1e-5f));
                wsync();
                stl<float>(sb + 256 + r * 4, rho);
                wsync();
                const f32x4 rr = ldl<f32x4>(sb + 256 + g * 16);
                const u32x4 u0 = {pk(U[0][0], U[0][1]), pk(U[0][2], U[0][3]), pk(U[1][0], U[1][1]), pk(U[1][2], U[1][3])};
                const u32x4 u1 = {pk(U[2][0], U[2][1]), pk(U[2][2], U[2][3]), pk(U[3][0], U[3][1]), pk(U[3][2], U[3][3])};
                f32x4 S = MM32(x0, u0, zero); S = MM32(x1, u1, S);
                u32x4 A4[4];
#pragma unroll
                for (int et = 0; et < 4; ++et) {
                    f32x4 Z = MM32(x0, ldl<u32x4>(awf + et * 2048), zero); Z = MM32(x1, ldl<u32x4>(awf + et * 2048 + 1024), Z);
                    const u32x2 xt = et == 0 ? xt0 : et == 1 ? xt1 : et == 2 ? xt2 : xt3;
                    A4[et] = (u32x4){pk(Z[0], Z[1]), pk(Z[2], Z[3]), xt[0], xt[1]};
                }
                float p[4], o;
#pragma unroll
                for (int i = 0; i < 4; ++i) p[i] = S[i] * rr[i];
                float mx = swap16(fmaxf(fmaxf(p[0], p[1]), fmaxf(p[2], p[3])), o);
                mx = max2(mx, o) * rho;
#pragma unroll
                for (int i = 0; i < 4; ++i) p[i] = __builtin_amdgcn_exp2f(fmaf(p[i], rho, -mx));
                float ps = swap16(keep(p[0] + p[1]) + keep(p[2] + p[3]), o);
                const float inv = valid ? __builtin_amdgcn_rcpf(ps + o) : 0.f;
#pragma unroll
                for (int i = 0; i < 4; ++i) p[i] = keep(keep(p[i] * rr[i]) * inv);
                const u32x4 PB = {pk(p[0], p[1]), pk(p[2], p[3]), id0, id1};
                wsync();
                stl<u32x2>(sb + r * 32 + ((g ^ (r >> 2)) * 8), (u32x2){PB[0], PB[1]});
                wsync();
                const u32x2 PT = ldtr(sb + ((lane ^ g) * 8));
                f32x4 O[4];
#pragma unroll
                for (int et = 0; et < 4; ++et) O[et] = MM32(A4[et], PB, zero);
                const u32x4 o0 = {pk(O[0][0], O[0][1]), pk(O[0][2], O[0][3]), pk(O[1][0], O[1][1]), pk(O[1][2], O[1][3])};
                const u32x4 o1 = {pk(O[2][0], O[2][1]), pk(O[2][2], O[2][3]), pk(O[3][0], O[3][1]), pk(O[3][2], O[3][3])};
                claim();
                G = MM32(o0, o0, zero); G = MM32(o1, o1, G);
                mu = MM32(A2[0], o0, zero); mu = MM32(A2[1], o1, mu);
                wsync();
                if (diag) stl<f32x4>(sb + r * 16, G);
                wsync();
                const float q2 = ldl<float>(sb + r * 16 + (r & 3) * 4);
                const float rho2 = __builtin_amdgcn_rsqf(fmaxf(fmaf(q2, 1.f / 64, fmaf(-mu[0], mu[0], 1e-5f)), 1e-5f));
                const float z = fmaf(-mu[0], sw, mu[1]) * 1.44269504088896f * rho2;
                const float ez = __builtin_amdgcn_exp2f(z - max8(z));
                const float al = ez * __builtin_amdgcn_rcpf(sum8(ez));
                wsync();
                stl<float>(sb + 256 + r * 4, al);
                wsync();
                const f32x4 ar = ldl<f32x4>(sb + 256 + g * 16);
                const u32x2 AB = {valid ? pk(ar[0], ar[1]) : 0u, valid ? pk(ar[2], ar[3]) : 0u};
                const f32x4 be = MM16(PT, AB, zero);
                const u32x4 FB = {pk(be[0], be[1]), pk(be[2], be[3]), AB[0], AB[1]};
#pragma unroll
                for (int et = 0; et < 4; ++et) O[et] = MM32(A4[et], FB, zero);
                const u32x4 f0 = {pk(O[0][0], O[0][1]), pk(O[0][2], O[0][3]), pk(O[1][0], O[1][1]), pk(O[1][2], O[1][3])};
                const u32x4 f1 = {pk(O[2][0], O[2][1]), pk(O[2][2], O[2][3]), pk(O[3][0], O[3][1]), pk(O[3][2], O[3][3])};
                const u32 oo = ((lane & 7) == 0 && (int)dl < nloc) ? (u32)(b * NPB + dl) * 128u + g * 16u : PADOFF;
                __builtin_amdgcn_raw_buffer_store_b128(f0, mkrs(out, TB), oo, 0, OA);
                __builtin_amdgcn_raw_buffer_store_b128(f1, mkrs(out, TB), oo + 64, 0, OA);
                wsync();
            }
        }
    }
    STP(11);
}

__global__ void x_hist(const int* dst, int E, int* cnt) {
    const int i = blockIdx.x * blockDim.x + threadIdx.x;
    if (i < E) atomicAdd(&cnt[dst[i]], 1);
}
__global__ void __launch_bounds__(1024) x_scan(const int* cnt, int N, int* rowptr, int* cursor) {
    __shared__ int part[1024];
    const int t = threadIdx.x, per = (N + 1023) / 1024;
    int s = 0;
    for (int i = t * per; i < min(N, (t + 1) * per); ++i) s += cnt[i];
    part[t] = s;
    __syncthreads();
    for (int o = 1; o < 1024; o <<= 1) {
        const int v = t >= o ? part[t - o] : 0;
        __syncthreads();
        part[t] += v;
        __syncthreads();
    }
    int base = part[t] - s;
    for (int i = t * per; i < min(N, (t + 1) * per); ++i) { rowptr[i] = base; cursor[i] = base; base += cnt[i]; }
    if (t == 1023) rowptr[N] = part[1023];
}
__global__ void x_fill(const int* src, const int* dst, int E, int* cursor, int* col) {
    const int i = blockIdx.x * blockDim.x + threadIdx.x;
    if (i < E) col[atomicAdd(&cursor[dst[i]], 1)] = src[i];
}
__global__ void x_g0(const u16* x, const u16* dis, int N, u16* g) {
    const size_t i = (size_t)blockIdx.x * blockDim.x + threadIdx.x;
    if (i < (size_t)N * 64) g[i] = f2bf(bf2f(x[i]) * bf2f(dis[i >> 6]));
}
__global__ void x_hop(const u16* gin, const int* rowptr, const int* col, const u16* dis, int N, u16* hout, u16* gout) {
    const int n = blockIdx.x * 4 + (threadIdx.x >> 6), lane = threadIdx.x & 63;
    if (n >= N) return;
    float acc = 0.f;
    for (int e = rowptr[n]; e < rowptr[n + 1]; ++e) acc += bf2f(gin[(size_t)col[e] * 64 + lane]);
    const float d = bf2f(dis[n]);
    const u16 h = f2bf(acc * d);
    hout[(size_t)n * 64 + lane] = h;
    gout[(size_t)n * 64 + lane] = f2bf(bf2f(h) * d);
}
__device__ __forceinline__ float wsum(float v) {
#pragma unroll
    for (int o = 32; o; o >>= 1) v += __shfl_xor(v, o);
    return v;
}
__global__ void __launch_bounds__(256) x_final(const u16* x, const u16* hs, int N, const u16* wq, const u16* wk, const u16* wv,
                                               const u16* wo, const u16* wr, u16* out) {
    __shared__ u16 sw[4][64 * 64];
    __shared__ float st[4][8][64];
    for (int i = threadIdx.x; i < 4096; i += 256) { sw[0][i] = wq[i]; sw[1][i] = wk[i]; sw[2][i] = wv[i]; sw[3][i] = wo[i]; }
    __syncthreads();
    const int wid = threadIdx.x >> 6, lane = threadIdx.x & 63;
    const float wrl = bf2f(wr[lane]);
    for (int n0 = blockIdx.x * 4; n0 < N; n0 += gridDim.x * 4) {
        const int n = n0 + wid, nn = min(n, N - 1);
        float tk[8], q[8], kk[8], vv[8], o[8], z[8];
#pragma unroll
        for (int k = 0; k < 8; ++k) {
            const u16* p = k == 0 ? x : hs + (size_t)(k - 1) * N * 64;
            tk[k] = bf2f(p[(size_t)nn * 64 + lane]);
            const float c = tk[k] - wsum(tk[k]) * (1.f / 64);
            st[wid][k][lane] = c / sqrtf(wsum(c * c) * (1.f / 64) + 1e-5f);
            q[k] = 0; kk[k] = 0; vv[k] = 0;
        }
        __syncthreads();
        for (int d = 0; d < 64; ++d) {
            const float a = bf2f(sw[0][d * 64 + lane]), b = bf2f(sw[1][d * 64 + lane]), c = bf2f(sw[2][d * 64 + lane]);
#pragma unroll
            for (int k = 0; k < 8; ++k) { const float t = st[wid][k][d]; q[k] += t * a; kk[k] += t * b; vv[k] += t * c; }
        }
#pragma unroll
        for (int k = 0; k < 8; ++k) {
            float s[8], mx = -3.0e38f, sum = 0, acc = 0;
#pragma unroll
            for (int l = 0; l < 8; ++l) { s[l] = wsum(q[k] * kk[l]) * 0.125f; mx = fmaxf(mx, s[l]); }
#pragma unroll
            for (int l = 0; l < 8; ++l) { s[l] = __expf(s[l] - mx); sum += s[l]; acc += s[l] * vv[l]; }
            o[k] = acc / sum;
        }
        __syncthreads();
#pragma unroll
        for (int k = 0; k < 8; ++k) st[wid][k][lane] = o[k];
        __syncthreads();
        for (int d = 0; d < 64; ++d) {
            const float a = bf2f(sw[3][d * 64 + lane]);
#pragma unroll
            for (int k = 0; k < 8; ++k) tk[k] += st[wid][k][d] * a;
        }
        float mx = -3.0e38f, sum = 0, res = 0;
#pragma unroll
        for (int k = 0; k < 8; ++k) {
            const float c = tk[k] - wsum(tk[k]) * (1.f / 64);
            z[k] = wsum(c / sqrtf(wsum(c * c) * (1.f / 64) + 1e-5f) * wrl);
            mx = fmaxf(mx, z[k]);
        }
#pragma unroll
        for (int k = 0; k < 8; ++k) { z[k] = __expf(z[k] - mx); sum += z[k]; res += z[k] * tk[k]; }
        if (n < N) out[(size_t)n * 64 + lane] = f2bf(res / sum);
        __syncthreads();
    }
}

extern "C" void kernel_launch(void* const* d_in, const int* in_sizes, int n_in,
                              void* d_out, int out_size, void* d_ws, size_t ws_size,
                              hipStream_t stream) {
    const u16* x = (const u16*)d_in[0];
    const int* src = (const int*)d_in[1];
    const int* dst = (const int*)d_in[2];
    const u16* dis = (const u16*)d_in[3];
    const u16* wq = (const u16*)d_in[4];
    const u16* wk = (const u16*)d_in[5];
    const u16* wv = (const u16*)d_in[6];
    const u16* wo = (const u16*)d_in[7];
    const u16* wr = (const u16*)d_in[8];
    const int N = in_sizes[3];
    const int E = in_sizes[1];
    char* ws = (char*)d_ws;
    size_t off = 0;
    auto alloc = [&](size_t bytes) { void* p = ws + off; off += (bytes + 4095) & ~(size_t)4095; return p; };
    const int chunk = ((E + NWG - 1) / NWG + 3) & ~3;
    if (N <= 0) return;
    if (N > NWG * NPB || chunk > 4096 || ws_size < (size_t)TS * HOPS + (size_t)NWG * NWG * 128 + (size_t)NWG * 4096 * 4 + 65536 FORCEGEN) {
        int* cnt = (int*)alloc((size_t)N * 4);
        int* rowptr = (int*)alloc((size_t)(N + 1) * 4);
        int* cursor = (int*)alloc((size_t)N * 4);
        int* col = (int*)alloc((size_t)E * 4 + 4);
        u16* ga = (u16*)alloc((size_t)N * 128);
        u16* gb = (u16*)alloc((size_t)N * 128);
        u16* hs = (u16*)alloc((size_t)N * 128 * HOPS);
        (void)hipMemsetAsync(cnt, 0, (size_t)N * 4, stream);
        x_hist<<<E / 256 + 1, 256, 0, stream>>>(dst, E, cnt);
        x_scan<<<1, 1024, 0, stream>>>(cnt, N, rowptr, cursor);
        x_fill<<<E / 256 + 1, 256, 0, stream>>>(src, dst, E, cursor, col);
        x_g0<<<(N + 3) / 4, 256, 0, stream>>>(x, dis, N, ga);
        for (int k = 0; k < HOPS; ++k) {
            x_hop<<<(N + 3) / 4, 256, 0, stream>>>(ga, rowptr, col, dis, N, hs + (size_t)k * N * 64, gb);
            u16* tmp = ga; ga = gb; gb = tmp;
        }
        x_final<<<512, 256, 0, stream>>>(x, hs, N, wq, wk, wv, wo, wr, (u16*)d_out);
        return;
    }
    char* gtab = (char*)alloc((size_t)TS * HOPS);
    u32x4* cells = (u32x4*)alloc((size_t)NWG * NWG * 128);
    u32* ovf = (u32*)alloc((size_t)NWG * chunk * 4);
    u32* ovh = (u32*)alloc(NWG * 64);
    k_bin<<<NWG, 1024, 0, stream>>>(src, dst, x, dis, cells, ovf, ovh, (u32x4*)gtab, E, chunk, N
    );
    k_main<<<NWG, 1024, 0, stream>>>(cells, ovf, ovh, chunk, N, gtab, x, dis, wq, wk, wv, wo, wr, (u16*)d_out
    );
}
```
